```python
import torch

def _softmax(x, axis=-1):
    return torch.softmax(x, dim=axis)
def _gelu(x):
    return torch.nn.functional.gelu(x, approximate="tanh")
def _silu(x):
    return torch.nn.functional.silu(x)
def _sigmoid(x):
    return torch.sigmoid(x)
def _layernorm(x, axis=-1, eps=1e-5):
    m = x.mean(dim=axis, keepdim=True)
    v = x.var(dim=axis, keepdim=True, unbiased=False)
    return (x - m) / torch.sqrt(v + eps)
def _scatter_add(tgt, idx, src, axis=0):
    return tgt.clone().index_add_(axis, idx, src)
def _segment_sum(src, seg, n):
    out = torch.zeros((n,) + src.shape[1:], dtype=src.dtype)
    return out.index_add_(0, seg, src)
def _pad(x, pw):
    flat = tuple(v for pair in reversed(pw) for v in pair)
    return torch.nn.functional.pad(x, flat)

kernel_name = 'recurrent_scan__gqa_block_cached__mixer_channel_mlp__decode'

def setup_inputs():

    _w = (lambda a: (lambda t: t.to(torch.bfloat16) if t.is_floating_point() else t)(torch.as_tensor(a)))
    def _A_setup():
        B, Sq, D = 1, 1, 512
        Hq, Hkv, Sk, Dh = 32, 8, 32768, 128
        assert Hq % Hkv == 0, (Hq, Hkv)
        return {
            "x": _w((lambda *s: torch.randn(*s, dtype=torch.bfloat16) * 0.1)(B, Sq, D)),
            "k_cache": _w((lambda *s: torch.randn(*s, dtype=torch.bfloat16) * 0.8)(B, Hkv, Sk, Dh)),
            "v_cache": _w((lambda *s: torch.randn(*s, dtype=torch.bfloat16) * 3.2)(B, Hkv, Sk, Dh)),
            "wq": _w((lambda *s: torch.randn(*s, dtype=torch.bfloat16) * 0.8)(D, Hq * Dh) * (D ** -0.5)),
            "wo": _w((lambda *s: torch.randn(*s, dtype=torch.bfloat16) * 3.2)(Hq * Dh, D) * ((Hq * Dh) ** -0.5)),
        }
    _A_in = _A_setup()

    def _B_setup():
        B, S, D, F = 1, 1, 512, 1792
        return {
            "x": _w((lambda *s: torch.randn(*s, dtype=torch.bfloat16) * 0.1)(B, S, D)),
            "w1": _w((lambda *s: torch.randn(*s, dtype=torch.bfloat16) * 3.2)(D, F) * (D ** -0.5)),
            "b1": _w((lambda *s: torch.randn(*s, dtype=torch.bfloat16) * 3.2)(F)),
            "w2": _w((lambda *s: torch.randn(*s, dtype=torch.bfloat16) * 3.2)(F, D) * (F ** -0.5)),
            "b2": _w((lambda *s: torch.randn(*s, dtype=torch.bfloat16) * 3.2)(D)),
        }
    _B_in = _B_setup()

    return {
        "A_x": _A_in["x"],
        "A_k_cache": _A_in["k_cache"],
        "A_v_cache": _A_in["v_cache"],
        "A_wq": _A_in["wq"],
        "A_wo": _A_in["wo"],
        "B_x": _B_in["x"],
        "B_w1": _B_in["w1"],
        "B_b1": _B_in["b1"],
        "B_w2": _B_in["w2"],
        "B_b2": _B_in["b2"],
    }

def reference(A_x, A_k_cache, A_v_cache, A_wq, A_wo, B_x, B_w1, B_b1, B_w2, B_b2):
    def _A_ref(x, k_cache, v_cache, wq, wo):
        B, Sq, D = x.shape
        Hq, Dh = 32, 128
        Hkv = k_cache.shape[1]
        rep = Hq // Hkv
        q = (lambda x, *d: x.permute(*d))((x @ wq).reshape(B, Sq, Hq, Dh), 0, 2, 1, 3)
        k = (lambda x, n, axis: torch.repeat_interleave(x, n, dim=axis))(k_cache, rep, axis=1)
        v = (lambda x, n, axis: torch.repeat_interleave(x, n, dim=axis))(v_cache, rep, axis=1)
        scores = torch.einsum("bhqd,bhkd->bhqk", q, k) * (Dh ** -0.5)
        p = _softmax(scores, axis=-1)
        o = torch.einsum("bhqk,bhkd->bhqd", p, v)
        return (lambda x, *d: x.permute(*d))(o, 0, 2, 1, 3).reshape(B, Sq, Hq * Dh) @ wo

    def _B_ref(x, w1, b1, w2, b2):
        y = _layernorm(x, axis=-1)
        h = _gelu(torch.einsum("bsd,df->bsf", y, w1) + b1)
        return torch.einsum("bsf,fd->bsd", h, w2) + b2

    x = A_x
    xn = _layernorm(x, axis=-1)
    s = _A_ref(xn, A_k_cache, A_v_cache, A_wq, A_wo)
    g = _silu(_B_ref(xn, B_w1, B_b1, B_w2, B_b2))
    return x + s * g
```

```cpp
#include <hip/hip_runtime.h>
#include <hip/hip_bf16.h>
#include <stdint.h>

#define GAS __attribute__((address_space(1)))
#define LAS __attribute__((address_space(3)))
#define RLX_AGENT __ATOMIC_RELAXED, __HIP_MEMORY_SCOPE_AGENT
typedef unsigned u32x4 __attribute__((ext_vector_type(4)));
typedef unsigned u32x2 __attribute__((ext_vector_type(2)));
typedef float f32x4 __attribute__((ext_vector_type(4)));
typedef __bf16 bf16x2 __attribute__((ext_vector_type(2)));
typedef unsigned long long u64;
typedef GAS u64 gu64;
typedef GAS unsigned gu32;

constexpr int NB = 256, NT = 512, NW = 8;
constexpr int D = 512, HQ = 32, HKV = 8, SK = 32768, DH = 128, FF = 1792;
constexpr int NSPLIT = 32, KEYS_PER_BLOCK = SK / NSPLIT;
constexpr int NTILE = KEYS_PER_BLOCK / 16 / NW;
constexpr int NMLP = 224;
constexpr float C2 = 0.088388347648318440f * 1.4426950408889634f;
constexpr unsigned kSpinCap = 1u << 15;
constexpr int AUX_SC1 = 16;
constexpr int RSRC_W3 = 0x00020000;

constexpr int WS_NONCE = 0;
constexpr int WS_QG = 4096;
constexpr int WS_HG = WS_QG + 4096 * 8;
constexpr int WS_AML = 65536;
constexpr int WS_AACC = WS_AML + 32 * 32 * 16;
constexpr int WS_SG = WS_AACC + 32 * 32 * 1024;
constexpr int WS_MG = WS_SG + 8 * 32 * 64 * 8;
constexpr int WS_END = WS_MG + 8 * 28 * 64 * 8;

constexpr int L_XS0 = 0, L_XS1 = 512, L_QS = 1024, L_FLAG = 1280, L_P = 1296, L_MRG = L_P + NW * 64, MRG_REC = 132;
constexpr int L_H = L_MRG + NW * 4 * MRG_REC, L_T1 = L_H + 64, L_T2 = L_T1 + NW * MRG_REC, L_END = L_T2 + NW * 64;

struct Params {
  const uint16_t* x; const uint16_t* kc; const uint16_t* vc; const uint16_t* wq; const uint16_t* wo;
  const uint16_t* w1; const uint16_t* b1; const uint16_t* w2; const uint16_t* b2;
  uint16_t* out; unsigned char* ws;
};

__device__ __forceinline__ float bfr(float x) { return (float)(__bf16)x; }
__device__ __forceinline__ unsigned bfbits(float x) { return (unsigned)__builtin_bit_cast(unsigned short, (__bf16)x); }
__device__ __forceinline__ float lo16(unsigned u) { return __builtin_bit_cast(float, u << 16); }
__device__ __forceinline__ float hi16(unsigned u) { return __builtin_bit_cast(float, u & 0xffff0000u); }
__device__ __forceinline__ float dot2(unsigned k, unsigned q, float acc) {
  return __builtin_amdgcn_fdot2_f32_bf16(__builtin_bit_cast(bf16x2, k), __builtin_bit_cast(bf16x2, q), acc, false);
}
template <int CTRL> __device__ __forceinline__ float dpp(float x) {
  return __builtin_bit_cast(float, __builtin_amdgcn_mov_dpp(__builtin_bit_cast(int, x), CTRL, 0xf, 0xf, true));
}
constexpr int XOR1 = 0xB1, XOR2 = 0x4E, XOR7 = 0x141, XOR15 = 0x140, XOR8 = 0x128, ROR4 = 0x124;
__device__ __forceinline__ float xrow16_max(float x) {
  auto s = __builtin_amdgcn_permlane16_swap(__float_as_uint(x), __float_as_uint(x), false, false);
  x = fmaxf(__uint_as_float(s[0]), __uint_as_float(s[1]));
  auto t = __builtin_amdgcn_permlane32_swap(__float_as_uint(x), __float_as_uint(x), false, false);
  return fmaxf(__uint_as_float(t[0]), __uint_as_float(t[1]));
}
__device__ __forceinline__ float xrow16_sum(float x) {
  auto s = __builtin_amdgcn_permlane16_swap(__float_as_uint(x), __float_as_uint(x), false, false);
  x = __uint_as_float(s[0]) + __uint_as_float(s[1]);
  auto t = __builtin_amdgcn_permlane32_swap(__float_as_uint(x), __float_as_uint(x), false, false);
  return __uint_as_float(t[0]) + __uint_as_float(t[1]);
}
__device__ __forceinline__ float wave_sum(float x) {
  x += dpp<XOR1>(x); x += dpp<XOR2>(x); x += dpp<XOR7>(x); x += dpp<XOR15>(x);
  return xrow16_sum(x);
}
__device__ __forceinline__ void layernorm8(float (&v)[8]) {
  float s = 0.f;
#pragma unroll
  for (int i = 0; i < 8; ++i) s += v[i];
  const float mean = wave_sum(s) * (1.f / 512.f);
  float q = 0.f;
#pragma unroll
  for (int i = 0; i < 8; ++i) { const float d = v[i] - mean; q = fmaf(d, d, q); }
  const float var = wave_sum(q) * (1.f / 512.f);
  const float mb = bfr(mean), vb = bfr(var);
  const float den = bfr(sqrtf(bfr(vb + 1e-5f)));
#pragma unroll
  for (int i = 0; i < 8; ++i) v[i] = bfr(bfr(v[i] - mb) / den);
}
__device__ __forceinline__ void unpack8(u32x4 w, float (&f)[8]) {
#pragma unroll
  for (int k = 0; k < 4; ++k) { f[2 * k] = lo16(w[k]); f[2 * k + 1] = hi16(w[k]); }
}
__device__ __forceinline__ float sel8(const float (&a)[8], int i) {
  float a0 = a[0], a1 = a[1], a2 = a[2], a3 = a[3], a4 = a[4], a5 = a[5], a6 = a[6], a7 = a[7];
  asm volatile("" : "+v"(a0), "+v"(a1), "+v"(a2), "+v"(a3), "+v"(a4), "+v"(a5), "+v"(a6), "+v"(a7));
  const bool b0 = (i & 1) != 0, b1 = (i & 2) != 0, b2 = (i & 4) != 0;
  const float r0 = b0 ? a1 : a0, r1 = b0 ? a3 : a2, r2 = b0 ? a5 : a4, r3 = b0 ? a7 : a6;
  const float s0 = b1 ? r1 : r0, s1 = b1 ? r3 : r2;
  return b2 ? s1 : s0;
}
__device__ __forceinline__ u32x4 ldnt(const void* p) { return __builtin_nontemporal_load((const u32x4*)p); }
__device__ __forceinline__ u32x4 ldpl(const void* p) { return *(const GAS u32x4*)p; }
#define LDS_WAIT() asm volatile("s_waitcnt lgkmcnt(0)" ::: "memory")

__global__ void __launch_bounds__(NT, 2) mega(Params p) {
  __shared__ __attribute__((aligned(16))) float lds[L_END];
  LAS unsigned* ldsu = (LAS unsigned*)lds;
  const int b = blockIdx.x, tid = threadIdx.x, lane = tid & 63;
  const int wave = __builtin_amdgcn_readfirstlane(tid >> 6);
  const int g = b & 7, sp = b >> 3;
  const int r = lane >> 4, c = lane & 15;
  const __amdgpu_buffer_rsrc_t rs = __builtin_amdgcn_make_buffer_rsrc((void*)p.ws, 0, WS_END, RSRC_W3);
  if (tid == 0) ldsu[L_FLAG] = 0u;
  __syncthreads();

  u32x4 wv[16];
  u32x4 xv = {0u, 0u, 0u, 0u};
  u64 nonce = 0;
  unsigned b1v = 0;
#pragma unroll
  for (int j = 0; j < 16; ++j) wv[j] = u32x4{0u, 0u, 0u, 0u};
  if (wave == 0) {
    const u32x4 z = {0u, 0u, 0u, 0u};
#pragma unroll
    for (int h = 0; h < 4; ++h)
      __builtin_amdgcn_raw_buffer_store_b128(z, rs, WS_AACC + ((g * 4 + h) * 32 + sp) * 1024 + lane * 16, 0, AUX_SC1);
    if (lane < 4) __builtin_amdgcn_raw_buffer_store_b128(z, rs, WS_AML + ((g * 4 + lane) * 32 + sp) * 16, 0, AUX_SC1);
    if (lane < 32) __builtin_amdgcn_raw_buffer_store_b128(z, rs, WS_SG + (((sp >> 2) * 32) + g * 4 + (sp & 3)) * 512 + lane * 16, 0, AUX_SC1);
    if (b < NMLP && lane >= 32) __builtin_amdgcn_raw_buffer_store_b128(z, rs, WS_MG + ((b & 7) * 28 + (b >> 3)) * 512 + (lane - 32) * 16, 0, AUX_SC1);
    xv = ldpl(p.x + 8 * lane);
    nonce = __hip_atomic_load((gu64*)(p.ws + WS_NONCE), RLX_AGENT);
    const uint16_t* wb = p.wq + (size_t)(lane >> 1) * 4096 + g * 512 + sp * 16 + (lane & 1) * 8;
#pragma unroll
    for (int j = 0; j < 16; ++j) wv[j] = ldpl(wb + (size_t)j * 32 * 4096);
  } else if (wave == 1 && b < NMLP) {
    xv = ldpl(p.x + 8 * lane);
    nonce = __hip_atomic_load((gu64*)(p.ws + WS_NONCE), RLX_AGENT);
    b1v = p.b1[8 * b + (lane & 7)];
    const uint16_t* wb = p.w1 + (size_t)lane * FF + 8 * b;
#pragma unroll
    for (int j = 0; j < 8; ++j) wv[j] = ldpl(wb + (size_t)j * 64 * FF);
  } else if (wave == 2 && b < NMLP) {
    nonce = __hip_atomic_load((gu64*)(p.ws + WS_NONCE), RLX_AGENT);
    const uint16_t* wb = p.w2 + (size_t)(64 * (b >> 3) + (lane >> 3)) * D + 64 * (b & 7) + (lane & 7) * 8;
#pragma unroll
    for (int j = 0; j < 8; ++j) wv[j] = ldpl(wb + (size_t)j * 8 * D);
  } else if (wave == 7) {
    nonce = __hip_atomic_load((gu64*)(p.ws + WS_NONCE), RLX_AGENT);
  }
  __builtin_amdgcn_sched_barrier(0);
  const uint16_t* kb = p.kc + ((size_t)g * SK + (size_t)sp * KEYS_PER_BLOCK + wave * 16 + r) * DH + 8 * c;
  const uint16_t* vb = p.vc + ((size_t)g * SK + (size_t)sp * KEYS_PER_BLOCK + wave * 16 + r) * DH + 8 * c;
  u32x4 Kw[4][4], Vw[4][4];
#pragma unroll
  for (int t = 0; t < 4; ++t) {
#pragma unroll
    for (int i = 0; i < 4; ++i) Kw[t][i] = ldnt(kb + (size_t)(t * 128 + 4 * i) * DH);
#pragma unroll
    for (int i = 0; i < 4; ++i) Vw[t][i] = ldnt(vb + (size_t)(t * 128 + 4 * i) * DH);
  }
  const int hl = sp & 3, cg = sp >> 2, head1 = g * 4 + hl;
  u32x4 wo2[2];
#pragma unroll
  for (int j = 0; j < 2; ++j) wo2[j] = ldpl(p.wo + (size_t)(head1 * 128 + 16 * wave + 8 * j + (lane >> 3)) * D + cg * 64 + (lane & 7) * 8);
  __builtin_amdgcn_sched_barrier(0);

  const u64 tag48 = ((nonce + 1ull) & 0xFFFFFFFFFFFFull) << 16;
  if (wave == 0) {
    float v[8];
    unpack8(xv, v);
    layernorm8(v);
    *(LAS f32x4*)(lds + L_XS0 + 8 * lane) = f32x4{v[0], v[1], v[2], v[3]};
    *(LAS f32x4*)(lds + L_XS0 + 8 * lane + 4) = f32x4{v[4], v[5], v[6], v[7]};
    LDS_WAIT();
    float a[8];
#pragma unroll
    for (int k = 0; k < 8; ++k) a[k] = 0.f;
#pragma unroll
    for (int j = 0; j < 16; ++j) {
      const float xr = lds[L_XS0 + 32 * j + (lane >> 1)];
      float f[8]; unpack8(wv[j], f);
#pragma unroll
      for (int k = 0; k < 8; ++k) a[k] = fmaf(xr, f[k], a[k]);
    }
#pragma unroll
    for (int k = 0; k < 8; ++k) { float t = a[k]; t += dpp<XOR2>(t); t += dpp<ROR4>(t); t += dpp<XOR8>(t); a[k] = xrow16_sum(t); }
    if (lane < 16) {
      const float qv = sel8(a, lane >> 1);
      const int col = (lane & 1) * 8 + (lane >> 1);
      __hip_atomic_store((gu64*)(p.ws + WS_QG + (g * 512 + sp * 16 + col) * 8), tag48 | (u64)bfbits(qv), RLX_AGENT);
    }
    for (unsigned spins = 0;; ++spins) {
      u32x4 qg[4];
#pragma unroll
      for (int j = 0; j < 4; ++j) qg[j] = __builtin_amdgcn_raw_buffer_load_b128(rs, WS_QG + (g * 512 + j * 128 + lane * 2) * 8, 0, AUX_SC1);
      bool ok = true;
      const unsigned tlo = (unsigned)(tag48 >> 16) & 0xFFFFu, thi = (unsigned)(tag48 >> 32);
#pragma unroll
      for (int j = 0; j < 4; ++j) ok &= ((qg[j][0] >> 16) == tlo) & (qg[j][1] == thi) & ((qg[j][2] >> 16) == tlo) & (qg[j][3] == thi);
      if (__all(ok) || spins > kSpinCap) {
#pragma unroll
        for (int j = 0; j < 4; ++j) ldsu[L_QS + j * 64 + lane] = (qg[j][0] & 0xFFFFu) | (qg[j][2] << 16);
        break;
      }
      __builtin_amdgcn_s_sleep(2);
    }
    LDS_WAIT();
    if (lane == 0) __hip_atomic_store(ldsu + L_FLAG, 1u, __ATOMIC_RELAXED, __HIP_MEMORY_SCOPE_WORKGROUP);
  } else if (wave == 1 && b < NMLP) {
    float v[8];
    unpack8(xv, v);
    layernorm8(v);
    layernorm8(v);
    *(LAS f32x4*)(lds + L_XS1 + 8 * lane) = f32x4{v[0], v[1], v[2], v[3]};
    *(LAS f32x4*)(lds + L_XS1 + 8 * lane + 4) = f32x4{v[4], v[5], v[6], v[7]};
    LDS_WAIT();
    float a[8];
#pragma unroll
    for (int k = 0; k < 8; ++k) a[k] = 0.f;
#pragma unroll
    for (int j = 0; j < 8; ++j) {
      const float xr = lds[L_XS1 + 64 * j + lane];
      float f[8]; unpack8(wv[j], f);
#pragma unroll
      for (int k = 0; k < 8; ++k) a[k] = fmaf(xr, f[k], a[k]);
    }
#pragma unroll
    for (int k = 0; k < 8; ++k) a[k] = wave_sum(a[k]);
    if (lane < 8) {
      const float u = bfr(bfr(sel8(a, lane)) + lo16(b1v));
      const float inner = 0.7978845608028654f * (u + 0.044715f * u * u * u);
      const float hv = 0.5f * u * (1.f + tanhf(inner));
      __hip_atomic_store((gu64*)(p.ws + WS_HG + (8 * b + lane) * 8), tag48 | (u64)bfbits(hv), RLX_AGENT);
    }
  } else if (wave == 2 && b < NMLP) {
    const int rg = b >> 3, mcg = b & 7;
    for (unsigned spins = 0;; ++spins) {
      const u64 hg = __hip_atomic_load((gu64*)(p.ws + WS_HG + (64 * rg + lane) * 8), RLX_AGENT);
      const bool ok = (hg & ~0xFFFFull) == tag48;
      if (__all(ok) || spins > kSpinCap) { lds[L_H + lane] = lo16((unsigned)hg & 0xFFFFu); break; }
      __builtin_amdgcn_s_sleep(2);
    }
    LDS_WAIT();
    float a[8];
#pragma unroll
    for (int k = 0; k < 8; ++k) a[k] = 0.f;
#pragma unroll
    for (int j = 0; j < 8; ++j) {
      const float hr = lds[L_H + 8 * j + (lane >> 3)];
      float f[8]; unpack8(wv[j], f);
#pragma unroll
      for (int k = 0; k < 8; ++k) a[k] = fmaf(hr, f[k], a[k]);
    }
#pragma unroll
    for (int k = 0; k < 8; ++k) { float t = a[k]; t += dpp<XOR8>(t); a[k] = xrow16_sum(t); }
    const float mv = sel8(a, lane >> 3);
    const int col = (lane & 7) * 8 + (lane >> 3);
    __hip_atomic_store((gu64*)(p.ws + WS_MG + ((mcg * 28 + rg) * 64 + col) * 8), (1ull << 32) | (u64)__float_as_uint(mv), RLX_AGENT);
  }
  for (unsigned spins = 0; spins < (1u << 24); ++spins) {
    if (__hip_atomic_load(ldsu + L_FLAG, __ATOMIC_RELAXED, __HIP_MEMORY_SCOPE_WORKGROUP) != 0u) break;
    __builtin_amdgcn_s_sleep(1);
  }
  asm volatile("" ::: "memory");
  u32x4 qpk[4];
#pragma unroll
  for (int h = 0; h < 4; ++h) qpk[h] = *(LAS u32x4*)(ldsu + L_QS + h * 64 + 4 * c);

  float m = -INFINITY, l = 0.f, acc[32];
#pragma unroll
  for (int k = 0; k < 32; ++k) acc[k] = 0.f;
  float* prow = lds + L_P + (wave * 4 + r) * 16;
#pragma unroll
  for (int t = 0; t < NTILE; ++t) {
    const int sl = t & 3;
    float x[16];
#pragma unroll
    for (int i = 0; i < 4; ++i)
#pragma unroll
      for (int h = 0; h < 4; ++h) {
        float d = 0.f;
#pragma unroll
        for (int k = 0; k < 4; ++k) d = dot2(Kw[sl][i][k], qpk[h][k], d);
        x[i * 4 + h] = d;
      }
    if (t + 4 < NTILE) {
#pragma unroll
      for (int i = 0; i < 4; ++i) Kw[sl][i] = ldnt(kb + (size_t)((t + 4) * 128 + 4 * i) * DH);
    }
    __builtin_amdgcn_sched_barrier(0);
#define TR_STEP(HALF, CTRL, BIT) _Pragma("unroll") for (int h = 0; h < (HALF); ++h) { \
      const float lo_ = x[h], hi_ = x[h + (HALF)]; const float t1_ = lo_ + dpp<CTRL>(lo_), t2_ = hi_ + dpp<CTRL>(hi_); \
      x[h] = (c & (BIT)) ? t2_ : t1_; }
    TR_STEP(8, XOR8, 8) TR_STEP(4, XOR7, 4) TR_STEP(2, XOR2, 2) TR_STEP(1, XOR1, 1)
#undef TR_STEP
    const float sc = x[0] * C2;
    float pm = sc; pm = fmaxf(pm, dpp<ROR4>(pm)); pm = fmaxf(pm, dpp<XOR8>(pm)); pm = xrow16_max(pm);
    const float mn = fmaxf(m, pm), msub = (mn == -INFINITY) ? 0.f : mn;
    const float alpha = __builtin_amdgcn_exp2f(m - msub), pv = __builtin_amdgcn_exp2f(sc - msub);
    l = fmaf(l, alpha, pv); m = mn;
    prow[c] = pv;
    {
      const float a0 = dpp<0x00>(alpha), a1 = dpp<0x55>(alpha), a2 = dpp<0xAA>(alpha), a3 = dpp<0xFF>(alpha);
#pragma unroll
      for (int k = 0; k < 8; ++k) { acc[k] *= a0; acc[8 + k] *= a1; acc[16 + k] *= a2; acc[24 + k] *= a3; }
    }
    LDS_WAIT();
    f32x4 pr[4];
#pragma unroll
    for (int u = 0; u < 4; ++u) pr[u] = *(LAS f32x4*)(prow + 4 * u);
#pragma unroll
    for (int i = 0; i < 4; ++i) {
      float vf[8]; unpack8(Vw[sl][i], vf);
#pragma unroll
      for (int h = 0; h < 4; ++h) {
        const float pg = pr[i][h];
#pragma unroll
        for (int k = 0; k < 8; ++k) acc[h * 8 + k] = fmaf(pg, vf[k], acc[h * 8 + k]);
      }
    }
    if (t + 4 < NTILE) {
#pragma unroll
      for (int i = 0; i < 4; ++i) Vw[sl][i] = ldnt(vb + (size_t)((t + 4) * 128 + 4 * i) * DH);
    }
    __builtin_amdgcn_sched_barrier(0);
  }
#pragma unroll
  for (int k = 0; k < 32; ++k) acc[k] = xrow16_sum(acc[k]);
  { float t = l; t += dpp<ROR4>(t); t += dpp<XOR8>(t); l = xrow16_sum(t); }
  if (r == 0) {
#pragma unroll
    for (int h = 0; h < 4; ++h) {
      float* rec = lds + L_MRG + (wave * 4 + h) * MRG_REC;
      *(LAS f32x4*)(rec + 4 + 8 * c) = f32x4{acc[h * 8], acc[h * 8 + 1], acc[h * 8 + 2], acc[h * 8 + 3]};
      *(LAS f32x4*)(rec + 4 + 8 * c + 4) = f32x4{acc[h * 8 + 4], acc[h * 8 + 5], acc[h * 8 + 6], acc[h * 8 + 7]};
      if (c == h) { rec[0] = m; rec[1] = l; }
    }
  }
  __syncthreads();
  if (wave < 4) {
    const int h = wave;
    float M = -INFINITY;
#pragma unroll
    for (int w = 0; w < NW; ++w) M = fmaxf(M, lds[L_MRG + (w * 4 + h) * MRG_REC]);
    float L = 0.f, o0 = 0.f, o1 = 0.f;
#pragma unroll
    for (int w = 0; w < NW; ++w) {
      const float* rec = lds + L_MRG + (w * 4 + h) * MRG_REC;
      const float wt = __builtin_amdgcn_exp2f(rec[0] - M);
      L = fmaf(wt, rec[1], L); o0 = fmaf(wt, rec[4 + 2 * lane], o0); o1 = fmaf(wt, rec[5 + 2 * lane], o1);
    }
    const int slot = (g * 4 + h) * 32 + sp;
    __builtin_amdgcn_raw_buffer_store_b128(u32x4{__float_as_uint(o0), 1u, __float_as_uint(o1), 1u}, rs, WS_AACC + slot * 1024 + lane * 16, 0, AUX_SC1);
    if (lane == 0) __builtin_amdgcn_raw_buffer_store_b128(u32x4{__float_as_uint(M), 1u, __float_as_uint(L), 1u}, rs, WS_AML + slot * 16, 0, AUX_SC1);
  }
  const bool is_final = (hl == 0) && (g == cg);
  {
    float Mw, Lw, o0, o1;
    for (unsigned spins = 0;; ++spins) {
      u32x4 ml[4], ac[4];
#pragma unroll
      for (int j = 0; j < 4; ++j) {
        const int slot = head1 * 32 + wave * 4 + j;
        ml[j] = __builtin_amdgcn_raw_buffer_load_b128(rs, WS_AML + slot * 16, 0, AUX_SC1);
        ac[j] = __builtin_amdgcn_raw_buffer_load_b128(rs, WS_AACC + slot * 1024 + lane * 16, 0, AUX_SC1);
      }
      bool ok = true;
#pragma unroll
      for (int j = 0; j < 4; ++j) ok &= (ml[j][1] == 1u) & (ml[j][3] == 1u) & (ac[j][1] == 1u) & (ac[j][3] == 1u);
      if (__all(ok) || spins > kSpinCap) {
        Mw = -INFINITY;
#pragma unroll
        for (int j = 0; j < 4; ++j) Mw = fmaxf(Mw, __uint_as_float(ml[j][0]));
        Lw = 0.f; o0 = 0.f; o1 = 0.f;
#pragma unroll
        for (int j = 0; j < 4; ++j) {
          const float wt = __builtin_amdgcn_exp2f(__uint_as_float(ml[j][0]) - Mw);
          Lw = fmaf(wt, __uint_as_float(ml[j][2]), Lw);
          o0 = fmaf(wt, __uint_as_float(ac[j][0]), o0); o1 = fmaf(wt, __uint_as_float(ac[j][2]), o1);
        }
        break;
      }
      __builtin_amdgcn_s_sleep(1);
    }
    float* rec = lds + L_T1 + wave * MRG_REC;
    rec[4 + 2 * lane] = o0; rec[5 + 2 * lane] = o1;
    if (lane == 0) { rec[0] = Mw; rec[1] = Lw; }
  }
  if (is_final && wave == 7) {
    const u64 tg = tag48;
    for (unsigned spins = 0;; ++spins) {
      bool ok = true;
#pragma unroll
      for (int j = 0; j < 4; ++j) {
        const u64 q = __hip_atomic_load((gu64*)(p.ws + WS_QG + (j * 64 + lane) * 16 * 8), RLX_AGENT);
        ok &= (q & ~0xFFFFull) == tg;
      }
      if (__all(ok) || spins > kSpinCap) break;
      __builtin_amdgcn_s_sleep(1);
    }
    if (b == 0 && lane == 0) __hip_atomic_store((gu64*)(p.ws + WS_NONCE), nonce + 1ull, RLX_AGENT);
  }
  __syncthreads();
  {
    float M = -INFINITY;
#pragma unroll
    for (int w = 0; w < NW; ++w) M = fmaxf(M, lds[L_T1 + w * MRG_REC]);
    float L = 0.f, oa = 0.f, ob = 0.f;
    const int ra = 16 * wave + (lane >> 3), rb = ra + 8;
#pragma unroll
    for (int w = 0; w < NW; ++w) {
      const float* rec = lds + L_T1 + w * MRG_REC;
      const float wt = __builtin_amdgcn_exp2f(rec[0] - M);
      L = fmaf(wt, rec[1], L); oa = fmaf(wt, rec[4 + ra], oa); ob = fmaf(wt, rec[4 + rb], ob);
    }
    const float inv = 1.f / L;
    oa = bfr(oa * inv); ob = bfr(ob * inv);
    float fa[8], fb[8], a[8];
    unpack8(wo2[0], fa); unpack8(wo2[1], fb);
#pragma unroll
    for (int k = 0; k < 8; ++k) { float t = fmaf(ob, fb[k], oa * fa[k]); t += dpp<XOR8>(t); a[k] = xrow16_sum(t); }
    lds[L_T2 + wave * 64 + (lane & 7) * 8 + (lane >> 3)] = sel8(a, lane >> 3);
  }
  __syncthreads();
  if (wave == 0) {
    float s = 0.f;
#pragma unroll
    for (int w = 0; w < NW; ++w) s += lds[L_T2 + w * 64 + lane];
    __hip_atomic_store((gu64*)(p.ws + WS_SG + ((cg * 32 + head1) * 64 + lane) * 8), (1ull << 32) | (u64)__float_as_uint(s), RLX_AGENT);
  }
  if (!is_final) return;
  {
    float ss, mm;
    for (unsigned spins = 0;; ++spins) {
      u64 sv[4], mv[4];
#pragma unroll
      for (int j = 0; j < 4; ++j) {
        sv[j] = __hip_atomic_load((gu64*)(p.ws + WS_SG + ((cg * 32 + wave * 4 + j) * 64 + lane) * 8), RLX_AGENT);
        const int rg = wave + 8 * j;
        mv[j] = (rg < 28) ? __hip_atomic_load((gu64*)(p.ws + WS_MG + ((cg * 28 + rg) * 64 + lane) * 8), RLX_AGENT) : (1ull << 32);
      }
      bool ok = true;
#pragma unroll
      for (int j = 0; j < 4; ++j) ok &= ((sv[j] >> 32) == 1ull) & ((mv[j] >> 32) == 1ull);
      if (__all(ok) || spins > kSpinCap) {
        ss = 0.f; mm = 0.f;
#pragma unroll
        for (int j = 0; j < 4; ++j) { ss += __uint_as_float((unsigned)sv[j]); mm += __uint_as_float((unsigned)mv[j]); }
        break;
      }
      __builtin_amdgcn_s_sleep(1);
    }
    lds[L_MRG + wave * 128 + lane] = ss;
    lds[L_MRG + wave * 128 + 64 + lane] = mm;
  }
  __syncthreads();
  if (wave == 0) {
    float s = 0.f, mlp = 0.f;
#pragma unroll
    for (int w = 0; w < NW; ++w) { s += lds[L_MRG + w * 128 + lane]; mlp += lds[L_MRG + w * 128 + 64 + lane]; }
    const int col = cg * 64 + lane;
    const float sb = bfr(s);
    const float u = bfr(bfr(mlp) + lo16(p.b2[col]));
    const float gv = bfr(u / (1.f + __expf(-u)));
    const float o = lo16(p.x[col]) + bfr(sb * gv);
    p.out[col] = (uint16_t)bfbits(o);
  }
}

extern "C" void kernel_launch(void* const* d_in, const int* in_sizes, int n_in, void* d_out, int out_size, void* d_ws, size_t ws_size,
                              hipStream_t stream) {
  Params p;
  p.x = (const uint16_t*)d_in[0]; p.kc = (const uint16_t*)d_in[1]; p.vc = (const uint16_t*)d_in[2];
  p.wq = (const uint16_t*)d_in[3]; p.wo = (const uint16_t*)d_in[4];
  p.w1 = (const uint16_t*)d_in[6]; p.b1 = (const uint16_t*)d_in[7]; p.w2 = (const uint16_t*)d_in[8]; p.b2 = (const uint16_t*)d_in[9];
  p.out = (uint16_t*)d_out; p.ws = (unsigned char*)d_ws;
  hipLaunchKernelGGL(mega, dim3(NB), dim3(NT), 0, stream, p);
}
```

```cpp
#include <hip/hip_runtime.h>
#include <hip/hip_bf16.h>
#include <stdint.h>

#define GAS __attribute__((address_space(1)))
#define LAS __attribute__((address_space(3)))
#define RLX_AGENT __ATOMIC_RELAXED, __HIP_MEMORY_SCOPE_AGENT
typedef unsigned u32x4 __attribute__((ext_vector_type(4)));
typedef unsigned u32x2 __attribute__((ext_vector_type(2)));
typedef float f32x4 __attribute__((ext_vector_type(4)));
typedef __bf16 bf16x2 __attribute__((ext_vector_type(2)));
typedef float f32x2 __attribute__((ext_vector_type(2)));
typedef short bf16x8 __attribute__((ext_vector_type(8)));
typedef unsigned long long u64;
typedef GAS u64 gu64;
typedef GAS unsigned gu32;

constexpr int NB = 256, NT = 512, NW = 8;
constexpr int D = 512, HQ = 32, HKV = 8, SK = 32768, DH = 128, FF = 1792;
constexpr int NSPLIT = 32, KEYS_PER_BLOCK = SK / NSPLIT;
constexpr int NTILE = KEYS_PER_BLOCK / 16 / NW;
constexpr int NMLP = 224;
constexpr float C2 = 0.088388347648318440f * 1.4426950408889634f;
#define CONTIG 0
constexpr int WOFF = CONTIG ? 128 : 16, TSTR = CONTIG ? 16 : 128;
#define POLL_SLEEP 4
#define QHI 1
#define QPRIO 1
#define QDELAY 4
constexpr unsigned kSpinCap = 1u << 15;
#define WIN_A 2
#define PACE_N 8
#define POLL_DELAY 0
constexpr int AUX_SC1 = 16;
constexpr int RSRC_W3 = 0x00020000;

constexpr int WS_NONCE = 0;
constexpr int WS_QG = 4096;
constexpr int WS_HG = WS_QG + 8 * 4 * 512 * 8;
constexpr int WS_AML = 262144;
constexpr int WS_AACC = WS_AML + 32 * 32 * 16;
constexpr int WS_SG = WS_AACC + 32 * 32 * 1024;
constexpr int WS_MG = WS_SG + 8 * 32 * 64 * 8;
constexpr int WS_END = WS_MG + 8 * 28 * 64 * 8;
constexpr int WS_STAMP = 4 << 20;
#define STAMP(k) do { } while (0)
#define STAMP_DUMP() do { } while (0)

constexpr int L_XS0 = 0, L_XS1 = 512, L_QS = 1024, L_FLAG = 1280, L_P = 1296, L_MRG = L_P + NW * 64, MRG_REC = 132;
constexpr int L_H = L_MRG + NW * 4 * MRG_REC, L_T1 = L_H + 64, L_T2 = L_T1 + NW * MRG_REC, L_T3 = L_T2 + NW * 64, L_QP = L_T3 + NW * 64, L_HP = L_QP + 4 * 64, L_END = L_HP + 4 * 64;

struct Params {
  const uint16_t* x; const uint16_t* kc; const uint16_t* vc; const uint16_t* wq; const uint16_t* wo;
  const uint16_t* w1; const uint16_t* b1; const uint16_t* w2; const uint16_t* b2;
  uint16_t* out; unsigned char* ws;
};

__device__ __forceinline__ float bfr(float x) { return (float)(__bf16)x; }
__device__ __forceinline__ unsigned bfbits(float x) { return (unsigned)__builtin_bit_cast(unsigned short, (__bf16)x); }
__device__ __forceinline__ float lo16(unsigned u) { return __builtin_bit_cast(float, u << 16); }
__device__ __forceinline__ float hi16(unsigned u) { return __builtin_bit_cast(float, u & 0xffff0000u); }
__device__ __forceinline__ float dot2(unsigned k, unsigned q, float acc) {
  return __builtin_amdgcn_fdot2_f32_bf16(__builtin_bit_cast(bf16x2, k), __builtin_bit_cast(bf16x2, q), acc, false);
}
template <int CTRL> __device__ __forceinline__ float dpp(float x) {
  return __builtin_bit_cast(float, __builtin_amdgcn_mov_dpp(__builtin_bit_cast(int, x), CTRL, 0xf, 0xf, true));
}
constexpr int XOR1 = 0xB1, XOR2 = 0x4E, XOR7 = 0x141, XOR15 = 0x140, XOR8 = 0x128, ROR4 = 0x124;
__device__ __forceinline__ float xrow16_max(float x) {
  auto s = __builtin_amdgcn_permlane16_swap(__float_as_uint(x), __float_as_uint(x), false, false);
  x = fmaxf(__uint_as_float(s[0]), __uint_as_float(s[1]));
  auto t = __builtin_amdgcn_permlane32_swap(__float_as_uint(x), __float_as_uint(x), false, false);
  return fmaxf(__uint_as_float(t[0]), __uint_as_float(t[1]));
}
__device__ __forceinline__ float xrow16_sum(float x) {
  auto s = __builtin_amdgcn_permlane16_swap(__float_as_uint(x), __float_as_uint(x), false, false);
  x = __uint_as_float(s[0]) + __uint_as_float(s[1]);
  auto t = __builtin_amdgcn_permlane32_swap(__float_as_uint(x), __float_as_uint(x), false, false);
  return __uint_as_float(t[0]) + __uint_as_float(t[1]);
}
__device__ __forceinline__ float wave_sum(float x) {
  x += dpp<XOR1>(x); x += dpp<XOR2>(x); x += dpp<XOR7>(x); x += dpp<XOR15>(x);
  return xrow16_sum(x);
}
__device__ __forceinline__ void layernorm8(float (&v)[8]) {
  float s = 0.f;
#pragma unroll
  for (int i = 0; i < 8; ++i) s += v[i];
  const float mean = wave_sum(s) * (1.f / 512.f);
  float q = 0.f;
#pragma unroll
  for (int i = 0; i < 8; ++i) { const float d = v[i] - mean; q = fmaf(d, d, q); }
  const float var = wave_sum(q) * (1.f / 512.f);
  const float mb = bfr(mean), vb = bfr(var);
  const float den = bfr(sqrtf(bfr(vb + 1e-5f)));
#pragma unroll
  for (int i = 0; i < 8; ++i) v[i] = bfr(bfr(v[i] - mb) / den);
}
__device__ __forceinline__ void unpack8(u32x4 w, float (&f)[8]) {
#pragma unroll
  for (int k = 0; k < 4; ++k) { f[2 * k] = lo16(w[k]); f[2 * k + 1] = hi16(w[k]); }
}
__device__ __forceinline__ float sel8(const float (&a)[8], int i) {
  float a0 = a[0], a1 = a[1], a2 = a[2], a3 = a[3], a4 = a[4], a5 = a[5], a6 = a[6], a7 = a[7];
  asm volatile("" : "+v"(a0), "+v"(a1), "+v"(a2), "+v"(a3), "+v"(a4), "+v"(a5), "+v"(a6), "+v"(a7));
  const bool b0 = (i & 1) != 0, b1 = (i & 2) != 0, b2 = (i & 4) != 0;
  const float r0 = b0 ? a1 : a0, r1 = b0 ? a3 : a2, r2 = b0 ? a5 : a4, r3 = b0 ? a7 : a6;
  const float s0 = b1 ? r1 : r0, s1 = b1 ? r3 : r2;
  return b2 ? s1 : s0;
}
__device__ __forceinline__ u32x4 ldnt(const void* p) { return __builtin_nontemporal_load((const u32x4*)p); }
__device__ __forceinline__ u32x4 ldpl(const void* p) { return __builtin_nontemporal_load((const u32x4*)p); }
#define LDS_WAIT() asm volatile("s_waitcnt lgkmcnt(0)" ::: "memory")

__global__ void __launch_bounds__(NT, 2) mega(Params p) {
  __shared__ __attribute__((aligned(16))) float lds[L_END];
  LAS unsigned* ldsu = (LAS unsigned*)lds;
  const int b = blockIdx.x, tid = threadIdx.x, lane = tid & 63;
  const int wave = __builtin_amdgcn_readfirstlane(tid >> 6);
  const int g = b & 7, sp = b >> 3;
  const int r = lane >> 4, c = lane & 15;
  const __amdgpu_buffer_rsrc_t rs = __builtin_amdgcn_make_buffer_rsrc((void*)p.ws, 0, WS_END, RSRC_W3);
  if (tid < 4) ldsu[L_FLAG + tid] = 0u;
  __syncthreads();

  const int hl = sp & 3, cg = sp >> 2, head1 = g * 4 + hl;
  const bool mlp = b < NMLP;
  const int hs = mlp ? (b & 7) * 28 + (b >> 3) : 0;
  const int rg2 = mlp ? (b >> 3) : 0;
  const bool qw = QHI ? wave >= 4 : wave < 4;
  const int qi = wave & 3;
  const uint16_t* kb = p.kc + ((size_t)g * SK + (size_t)sp * KEYS_PER_BLOCK + wave * WOFF + c) * DH + 8 * r;
  const uint16_t* vb = p.vc + ((size_t)g * SK + (size_t)sp * KEYS_PER_BLOCK + wave * WOFF + 4 * r) * DH + 8 * c;
  u32x4 Kw[4][4], Vw[4][4], w2v, wo2[2];
#define ISSUE_K(t) do { _Pragma("unroll") for (int i = 0; i < 4; ++i) Kw[t][i] = ldnt(kb + (size_t)(t) * TSTR * DH + 32 * i); __builtin_amdgcn_sched_barrier(0); } while (0)
#define ISSUE_V(t) do { _Pragma("unroll") for (int i = 0; i < 4; ++i) Vw[t][i] = ldnt(vb + (size_t)((t) * TSTR + i) * DH); __builtin_amdgcn_sched_barrier(0); } while (0)
  const u64 nonce = *(const GAS u64*)(p.ws + WS_NONCE);
  if (qw) {
    __builtin_amdgcn_s_setprio(3);
    const int jc = sp & 7, ks = sp >> 3;
    const int j1 = mlp ? b % 28 : 0, k1 = mlp ? b / 28 : 0;
    const u32x4 xv = *(const GAS u32x4*)(p.x + 8 * lane);
    u32x4 wqv[4], w1v[2];
#pragma unroll
    for (int j = 0; j < 4; ++j) wqv[j] = ldpl(p.wq + (size_t)(128 * ks + 32 * qi + 8 * j + (lane >> 3)) * 4096 + g * 512 + jc * 64 + (lane & 7) * 8);
    __builtin_amdgcn_sched_barrier(0);
    __builtin_amdgcn_s_barrier();
    __builtin_amdgcn_sched_barrier(0);
#pragma unroll
    for (int j = 0; j < 2; ++j) w1v[j] = ldpl(p.w1 + (size_t)(64 * k1 + 16 * qi + 8 * j + (lane >> 3)) * FF + j1 * 64 + (lane & 7) * 8);
    __builtin_amdgcn_sched_barrier(0);
    const u64 tg = (u64)(unsigned)(nonce + 1ull) << 32;
    float v[8];
    unpack8(xv, v);
    layernorm8(v);
    STAMP(10);
    *(LAS f32x4*)(lds + L_XS0 + 8 * lane) = f32x4{v[0], v[1], v[2], v[3]};
    *(LAS f32x4*)(lds + L_XS0 + 8 * lane + 4) = f32x4{v[4], v[5], v[6], v[7]};
    LDS_WAIT();
    float a[8];
#pragma unroll
    for (int k = 0; k < 8; ++k) a[k] = 0.f;
#pragma unroll
    for (int j = 0; j < 4; ++j) {
      const float xr = lds[L_XS0 + 128 * ks + 32 * qi + 8 * j + (lane >> 3)];
      float f[8]; unpack8(wqv[j], f);
#pragma unroll
      for (int k = 0; k < 8; ++k) a[k] = fmaf(xr, f[k], a[k]);
    }
#pragma unroll
    for (int k = 0; k < 8; ++k) { float t = a[k]; t += dpp<XOR8>(t); a[k] = xrow16_sum(t); }
    lds[L_QP + qi * 64 + (lane & 7) * 8 + (lane >> 3)] = sel8(a, lane >> 3);
    LDS_WAIT();
    STAMP(11);
    unsigned old = 0;
    if (lane == 0) old = __hip_atomic_fetch_add(ldsu + L_FLAG + 2, 1u, __ATOMIC_RELAXED, __HIP_MEMORY_SCOPE_WORKGROUP);
    old = __builtin_amdgcn_readfirstlane(old);
    asm volatile("" ::: "memory");
    if (old == 3) {
      float qv = 0.f;
#pragma unroll
      for (int w = 0; w < 4; ++w) qv += lds[L_QP + w * 64 + lane];
      __hip_atomic_store((gu64*)(p.ws + WS_QG + ((g * 4 + ks) * 512 + jc * 64 + lane) * 8), tg | (u64)__float_as_uint(qv), RLX_AGENT);
    }
    STAMP(1);
    if (qi == 0) {
      const unsigned t32 = (unsigned)(tg >> 32);
#pragma unroll
      for (int z = 0; z < QDELAY; ++z) __builtin_amdgcn_s_sleep(16);
      for (unsigned spins = 0;; ++spins) {
        u32x4 qg[4][4];
#pragma unroll
        for (int i = 0; i < 4; ++i)
#pragma unroll
          for (int k = 0; k < 4; ++k) qg[i][k] = __builtin_amdgcn_raw_buffer_load_b128(rs, WS_QG + ((g * 4 + k) * 512 + i * 128 + lane * 2) * 8, 0, AUX_SC1);
        bool ok = true;
#pragma unroll
        for (int i = 0; i < 4; ++i)
#pragma unroll
          for (int k = 0; k < 4; ++k) ok &= (qg[i][k][1] == t32) & (qg[i][k][3] == t32);
        if (__all(ok) || spins > kSpinCap) {
#pragma unroll
          for (int i = 0; i < 4; ++i) {
            const float q0 = (__uint_as_float(qg[i][0][0]) + __uint_as_float(qg[i][1][0])) + (__uint_as_float(qg[i][2][0]) + __uint_as_float(qg[i][3][0]));
            const float q1 = (__uint_as_float(qg[i][0][2]) + __uint_as_float(qg[i][1][2])) + (__uint_as_float(qg[i][2][2]) + __uint_as_float(qg[i][3][2]));
            ldsu[L_QS + i * 64 + lane] = bfbits(q0) | (bfbits(q1) << 16);
          }
          break;
        }
        __builtin_amdgcn_s_sleep(2);
      }
      LDS_WAIT();
      if (lane == 0) __hip_atomic_store(ldsu + L_FLAG, 1u, __ATOMIC_RELAXED, __HIP_MEMORY_SCOPE_WORKGROUP);
      STAMP(2);
    }
    layernorm8(v);
    *(LAS f32x4*)(lds + L_XS1 + 8 * lane) = f32x4{v[0], v[1], v[2], v[3]};
    *(LAS f32x4*)(lds + L_XS1 + 8 * lane + 4) = f32x4{v[4], v[5], v[6], v[7]};
    LDS_WAIT();
    {
      const float y0 = lds[L_XS1 + 64 * k1 + 16 * qi + (lane >> 3)], y1 = lds[L_XS1 + 64 * k1 + 16 * qi + 8 + (lane >> 3)];
      float f0[8], f1[8];
      unpack8(w1v[0], f0); unpack8(w1v[1], f1);
#pragma unroll
      for (int k = 0; k < 8; ++k) { float t = fmaf(y1, f1[k], y0 * f0[k]); t += dpp<XOR8>(t); a[k] = xrow16_sum(t); }
      lds[L_HP + qi * 64 + (lane & 7) * 8 + (lane >> 3)] = sel8(a, lane >> 3);
      LDS_WAIT();
      if (lane == 0) old = __hip_atomic_fetch_add(ldsu + L_FLAG + 1, 1u, __ATOMIC_RELAXED, __HIP_MEMORY_SCOPE_WORKGROUP);
      old = __builtin_amdgcn_readfirstlane(old);
      asm volatile("" ::: "memory");
      if (mlp && old == 3) {
        float hp = 0.f;
#pragma unroll
        for (int w = 0; w < 4; ++w) hp += lds[L_HP + w * 64 + lane];
        __hip_atomic_store((gu64*)(p.ws + WS_HG + ((j1 * 8 + k1) * 64 + lane) * 8), tg | (u64)__float_as_uint(hp), RLX_AGENT);
      }
    }
    __builtin_amdgcn_s_setprio(0);
  } else {
    __builtin_amdgcn_s_barrier();
  }
  __builtin_amdgcn_sched_barrier(0);
  ISSUE_K(0); ISSUE_V(0); ISSUE_K(1); ISSUE_V(1); ISSUE_K(2); ISSUE_V(2); ISSUE_K(3); ISSUE_V(3);
#pragma unroll
  for (int j = 0; j < 2; ++j) wo2[j] = ldpl(p.wo + (size_t)(head1 * 128 + 16 * wave + 8 * j + (lane >> 3)) * D + cg * 64 + (lane & 7) * 8);
  w2v = ldpl(p.w2 + (size_t)(64 * rg2 + 8 * wave + (lane >> 3)) * D + 64 * (b & 7) + (lane & 7) * 8);
  const unsigned b2v = p.b2[cg * 64 + lane], xo = p.x[cg * 64 + lane];
  __builtin_amdgcn_sched_barrier(0);
  const unsigned tag32 = (unsigned)(nonce + 1ull);
  const u64 tag32h = (u64)tag32 << 32;
  for (unsigned spins = 0; spins < (1u << 24); ++spins) {
    if (__hip_atomic_load(ldsu + L_FLAG, __ATOMIC_RELAXED, __HIP_MEMORY_SCOPE_WORKGROUP) != 0u) break;
    __builtin_amdgcn_s_sleep(1);
  }
  asm volatile("" ::: "memory");
  STAMP(3);
  u32x4 qf[4];
#pragma unroll
  for (int j = 0; j < 4; ++j) {
    const u32x4 t = *(LAS u32x4*)(ldsu + L_QS + (c & 3) * 64 + 16 * j + 4 * r);
    qf[j] = (c < 4) ? t : u32x4{0u, 0u, 0u, 0u};
  }

  float m = -INFINITY, l = 0.f;
  f32x2 acc[16];
#pragma unroll
  for (int k = 0; k < 16; ++k) acc[k] = f32x2{0.f, 0.f};
  float* pw = lds + L_P + wave * 64;
#pragma unroll
  for (int t = 0; t < NTILE; ++t) {
    const int sl = t & 3;
    f32x4 d = {0.f, 0.f, 0.f, 0.f};
#pragma unroll
    for (int j = 0; j < 4; ++j)
      d = __builtin_amdgcn_mfma_f32_16x16x32_bf16(__builtin_bit_cast(bf16x8, Kw[sl][j]), __builtin_bit_cast(bf16x8, qf[j]), d, 0, 0, 0);
    __builtin_amdgcn_sched_barrier(0);
    if (t + 4 < NTILE) {
#pragma unroll
      for (int i = 0; i < 4; ++i) Kw[sl][i] = ldnt(kb + (size_t)(t + 4) * TSTR * DH + 32 * i);
    }
    __builtin_amdgcn_sched_barrier(0);
    const float s0 = d[0] * C2, s1 = d[1] * C2, s2 = d[2] * C2, s3 = d[3] * C2;
    const float pm = xrow16_max(fmaxf(fmaxf(s0, s1), fmaxf(s2, s3)));
    const float mn = fmaxf(m, pm), msub = (mn == -INFINITY) ? 0.f : mn;
    const float alpha = __builtin_amdgcn_exp2f(m - msub);
    const f32x4 pv = {__builtin_amdgcn_exp2f(s0 - msub), __builtin_amdgcn_exp2f(s1 - msub), __builtin_amdgcn_exp2f(s2 - msub), __builtin_amdgcn_exp2f(s3 - msub)};
    l = fmaf(l, alpha, (pv[0] + pv[1]) + (pv[2] + pv[3])); m = mn;
    if (c < 4) *(LAS f32x4*)(pw + c * 16 + 4 * r) = pv;
#pragma unroll
    for (int h = 0; h < 4; ++h) {
      const float ah = __builtin_bit_cast(float, __builtin_amdgcn_readlane(__builtin_bit_cast(int, alpha), h));
#pragma unroll
      for (int k = 0; k < 4; ++k) acc[h * 4 + k] *= f32x2{ah, ah};
    }
    LDS_WAIT();
    f32x4 pr[4];
#pragma unroll
    for (int h = 0; h < 4; ++h) pr[h] = *(LAS f32x4*)(pw + h * 16 + 4 * r);
#pragma unroll
    for (int i = 0; i < 4; ++i) {
      f32x2 vf[4];
#pragma unroll
      for (int k = 0; k < 4; ++k) vf[k] = f32x2{lo16(Vw[sl][i][k]), hi16(Vw[sl][i][k])};
#pragma unroll
      for (int h = 0; h < 4; ++h) {
        const f32x2 pg = {pr[h][i], pr[h][i]};
#pragma unroll
        for (int k = 0; k < 4; ++k) acc[h * 4 + k] = __builtin_elementwise_fma(pg, vf[k], acc[h * 4 + k]);
      }
    }
    asm volatile("" : "+v"(acc[0]), "+v"(acc[1]), "+v"(acc[2]), "+v"(acc[3]), "+v"(acc[4]), "+v"(acc[5]), "+v"(acc[6]), "+v"(acc[7]),
                      "+v"(acc[8]), "+v"(acc[9]), "+v"(acc[10]), "+v"(acc[11]), "+v"(acc[12]), "+v"(acc[13]), "+v"(acc[14]), "+v"(acc[15]));
    __builtin_amdgcn_sched_barrier(0);
    if (t + 4 < NTILE) {
#pragma unroll
      for (int i = 0; i < 4; ++i) Vw[sl][i] = ldnt(vb + (size_t)((t + 4) * TSTR + i) * DH);
    }
    __builtin_amdgcn_sched_barrier(0);
  }
  STAMP(4);
#pragma unroll
  for (int k = 0; k < 16; ++k) acc[k] = f32x2{xrow16_sum(acc[k][0]), xrow16_sum(acc[k][1])};
  l = xrow16_sum(l);
  if (r == 0) {
#pragma unroll
    for (int h = 0; h < 4; ++h) {
      float* rec = lds + L_MRG + (wave * 4 + h) * MRG_REC;
      *(LAS f32x4*)(rec + 4 + 8 * c) = f32x4{acc[h * 4][0], acc[h * 4][1], acc[h * 4 + 1][0], acc[h * 4 + 1][1]};
      *(LAS f32x4*)(rec + 4 + 8 * c + 4) = f32x4{acc[h * 4 + 2][0], acc[h * 4 + 2][1], acc[h * 4 + 3][0], acc[h * 4 + 3][1]};
      if (c == h) { rec[0] = m; rec[1] = l; }
    }
  }
  __syncthreads();
  if (wave < 4) {
    const int h = wave;
    float M = -INFINITY;
#pragma unroll
    for (int w = 0; w < NW; ++w) M = fmaxf(M, lds[L_MRG + (w * 4 + h) * MRG_REC]);
    float L = 0.f, o0 = 0.f, o1 = 0.f;
#pragma unroll
    for (int w = 0; w < NW; ++w) {
      const float* rec = lds + L_MRG + (w * 4 + h) * MRG_REC;
      const float wt = __builtin_amdgcn_exp2f(rec[0] - M);
      L = fmaf(wt, rec[1], L); o0 = fmaf(wt, rec[4 + 2 * lane], o0); o1 = fmaf(wt, rec[5 + 2 * lane], o1);
    }
    const int slot = (g * 4 + h) * 32 + sp;
    __builtin_amdgcn_raw_buffer_store_b128(u32x4{__float_as_uint(o0), tag32, __float_as_uint(o1), tag32}, rs, WS_AACC + slot * 1024 + lane * 16, 0, AUX_SC1);
    if (lane == 0) __builtin_amdgcn_raw_buffer_store_b128(u32x4{__float_as_uint(M), tag32, __float_as_uint(L), tag32}, rs, WS_AML + slot * 16, 0, AUX_SC1);
  }
  STAMP(5);
  if (wave == 7 && mlp) {
    const unsigned b1v = __builtin_nontemporal_load(p.b1 + 64 * rg2 + lane);
    for (unsigned spins = 0;; ++spins) {
      u64 hg[8];
#pragma unroll
      for (int k = 0; k < 8; ++k) hg[k] = __hip_atomic_load((gu64*)(p.ws + WS_HG + ((rg2 * 8 + k) * 64 + lane) * 8), RLX_AGENT);
      bool ok = true;
#pragma unroll
      for (int k = 0; k < 8; ++k) ok &= (unsigned)(hg[k] >> 32) == tag32;
      if (__all(ok) || spins > kSpinCap) {
        float hp = 0.f;
#pragma unroll
        for (int k = 0; k < 8; ++k) hp += __uint_as_float((unsigned)hg[k]);
        const float u = bfr(bfr(hp) + lo16(b1v));
        const float inner = 0.7978845608028654f * (u + 0.044715f * u * u * u);
        lds[L_H + lane] = bfr(0.5f * u * (1.f + tanhf(inner)));
        break;
      }
      __builtin_amdgcn_s_sleep(1);
    }
  }
  const bool is_final = (hl == 0) && (g == cg);
  {
    float Mw, Lw, o0, o1;
    u32x4 mlv;
    for (unsigned spins = 0;; ++spins) {
      mlv = __builtin_amdgcn_raw_buffer_load_b128(rs, WS_AML + (head1 * 32 + wave * 4 + (lane & 3)) * 16, 0, AUX_SC1);
      if (__all((mlv[1] == tag32) & (mlv[3] == tag32)) || spins > kSpinCap) break;
      __builtin_amdgcn_s_sleep(POLL_SLEEP);
    }
    for (unsigned spins = 0;; ++spins) {
      u32x4 ac[4];
#pragma unroll
      for (int j = 0; j < 4; ++j) ac[j] = __builtin_amdgcn_raw_buffer_load_b128(rs, WS_AACC + (head1 * 32 + wave * 4 + j) * 1024 + lane * 16, 0, AUX_SC1);
      bool ok = true;
#pragma unroll
      for (int j = 0; j < 4; ++j) ok &= (ac[j][1] == tag32) & (ac[j][3] == tag32);
      if (__all(ok) || spins > kSpinCap) {
        float mj[4], lj[4];
#pragma unroll
        for (int j = 0; j < 4; ++j) {
          mj[j] = __builtin_bit_cast(float, __builtin_amdgcn_readlane((int)mlv[0], j));
          lj[j] = __builtin_bit_cast(float, __builtin_amdgcn_readlane((int)mlv[2], j));
        }
        Mw = fmaxf(fmaxf(mj[0], mj[1]), fmaxf(mj[2], mj[3]));
        Lw = 0.f; o0 = 0.f; o1 = 0.f;
#pragma unroll
        for (int j = 0; j < 4; ++j) {
          const float wt = __builtin_amdgcn_exp2f(mj[j] - Mw);
          Lw = fmaf(wt, lj[j], Lw);
          o0 = fmaf(wt, __uint_as_float(ac[j][0]), o0); o1 = fmaf(wt, __uint_as_float(ac[j][2]), o1);
        }
        break;
      }
      __builtin_amdgcn_s_sleep(1);
    }
    float* rec = lds + L_T1 + wave * MRG_REC;
    rec[4 + 2 * lane] = o0; rec[5 + 2 * lane] = o1;
    if (lane == 0) { rec[0] = Mw; rec[1] = Lw; }
  }
  STAMP(6);
  if (b == 0 && wave == 7) {
    for (unsigned spins = 0;; ++spins) {
      bool ok = true;
#pragma unroll
      for (int j = 0; j < 4; ++j) {
        const u64 q = __hip_atomic_load((gu64*)(p.ws + WS_QG + (j * 64 + lane) * 64 * 8), RLX_AGENT);
        ok &= (unsigned)(q >> 32) == tag32;
      }
      if (__all(ok) || spins > kSpinCap) break;
      __builtin_amdgcn_s_sleep(1);
    }
    if (lane == 0) __hip_atomic_store((gu64*)(p.ws + WS_NONCE), nonce + 1ull, RLX_AGENT);
  }
  __syncthreads();
  {
    float M = -INFINITY;
#pragma unroll
    for (int w = 0; w < NW; ++w) M = fmaxf(M, lds[L_T1 + w * MRG_REC]);
    float L = 0.f, oa = 0.f, ob = 0.f;
    const int ra = 16 * wave + (lane >> 3), rb = ra + 8;
#pragma unroll
    for (int w = 0; w < NW; ++w) {
      const float* rec = lds + L_T1 + w * MRG_REC;
      const float wt = __builtin_amdgcn_exp2f(rec[0] - M);
      L = fmaf(wt, rec[1], L); oa = fmaf(wt, rec[4 + ra], oa); ob = fmaf(wt, rec[4 + rb], ob);
    }
    const float inv = 1.f / L;
    oa = bfr(oa * inv); ob = bfr(ob * inv);
    float fa[8], fb[8], a[8];
    unpack8(wo2[0], fa); unpack8(wo2[1], fb);
#pragma unroll
    for (int k = 0; k < 8; ++k) { float t = fmaf(ob, fb[k], oa * fa[k]); t += dpp<XOR8>(t); a[k] = xrow16_sum(t); }
    lds[L_T2 + wave * 64 + (lane & 7) * 8 + (lane >> 3)] = sel8(a, lane >> 3);
    const float hr = lds[L_H + 8 * wave + (lane >> 3)];
    unpack8(w2v, fa);
#pragma unroll
    for (int k = 0; k < 8; ++k) { float t = hr * fa[k]; t += dpp<XOR8>(t); a[k] = xrow16_sum(t); }
    lds[L_T3 + wave * 64 + (lane & 7) * 8 + (lane >> 3)] = sel8(a, lane >> 3);
  }
  __syncthreads();
  if (wave == 0) {
    float s = 0.f;
#pragma unroll
    for (int w = 0; w < NW; ++w) s += lds[L_T2 + w * 64 + lane];
    __hip_atomic_store((gu64*)(p.ws + WS_SG + ((cg * 32 + head1) * 64 + lane) * 8), tag32h | (u64)__float_as_uint(s), RLX_AGENT);
  } else if (wave == 1 && mlp) {
    float s = 0.f;
#pragma unroll
    for (int w = 0; w < NW; ++w) s += lds[L_T3 + w * 64 + lane];
    __hip_atomic_store((gu64*)(p.ws + WS_MG + (((b & 7) * 28 + (b >> 3)) * 64 + lane) * 8), tag32h | (u64)__float_as_uint(s), RLX_AGENT);
  }
  STAMP(7);
  if (!is_final) { STAMP_DUMP(); return; }
  {
    float ss, mm;
    for (unsigned spins = 0;; ++spins) {
      u64 sv[4], mv[4];
#pragma unroll
      for (int j = 0; j < 4; ++j) {
        sv[j] = __hip_atomic_load((gu64*)(p.ws + WS_SG + ((cg * 32 + wave * 4 + j) * 64 + lane) * 8), RLX_AGENT);
        const int rg = wave + 8 * j;
        mv[j] = (rg < 28) ? __hip_atomic_load((gu64*)(p.ws + WS_MG + ((cg * 28 + rg) * 64 + lane) * 8), RLX_AGENT) : tag32h;
      }
      bool ok = true;
#pragma unroll
      for (int j = 0; j < 4; ++j) ok &= ((unsigned)(sv[j] >> 32) == tag32) & ((unsigned)(mv[j] >> 32) == tag32);
      if (__all(ok) || spins > kSpinCap) {
        ss = 0.f; mm = 0.f;
#pragma unroll
        for (int j = 0; j < 4; ++j) { ss += __uint_as_float((unsigned)sv[j]); mm += __uint_as_float((unsigned)mv[j]); }
        break;
      }
      __builtin_amdgcn_s_sleep(1);
    }
    STAMP(8);
    lds[L_MRG + wave * 128 + lane] = ss;
    lds[L_MRG + wave * 128 + 64 + lane] = mm;
  }
  __syncthreads();
  if (wave == 0) {
    float s = 0.f, mlp = 0.f;
#pragma unroll
    for (int w = 0; w < NW; ++w) { s += lds[L_MRG + w * 128 + lane]; mlp += lds[L_MRG + w * 128 + 64 + lane]; }
    const int col = cg * 64 + lane;
    const float sb = bfr(s);
    const float u = bfr(bfr(mlp) + lo16(b2v));
    const float gv = bfr(u / (1.f + __expf(-u)));
    const float o = lo16(xo) + bfr(sb * gv);
    p.out[col] = (uint16_t)bfbits(o);
    STAMP(9);
  }
  STAMP_DUMP();
}

extern "C" void kernel_launch(void* const* d_in, const int* in_sizes, int n_in, void* d_out, int out_size, void* d_ws, size_t ws_size,
                              hipStream_t stream) {
  Params p;
  p.x = (const uint16_t*)d_in[0]; p.kc = (const uint16_t*)d_in[1]; p.vc = (const uint16_t*)d_in[2];
  p.wq = (const uint16_t*)d_in[3]; p.wo = (const uint16_t*)d_in[4];
  p.w1 = (const uint16_t*)d_in[6]; p.b1 = (const uint16_t*)d_in[7]; p.w2 = (const uint16_t*)d_in[8]; p.b2 = (const uint16_t*)d_in[9];
  p.out = (uint16_t*)d_out; p.ws = (unsigned char*)d_ws;
  hipLaunchKernelGGL(mega, dim3(NB), dim3(NT), 0, stream, p);
}
```

```cpp
#include <hip/hip_runtime.h>
#include <hip/hip_bf16.h>
#include <stdint.h>

#define GAS __attribute__((address_space(1)))
#define LAS __attribute__((address_space(3)))
#define RLX_AGENT __ATOMIC_RELAXED, __HIP_MEMORY_SCOPE_AGENT
typedef unsigned u32x4 __attribute__((ext_vector_type(4)));
typedef unsigned u32x2 __attribute__((ext_vector_type(2)));
typedef float f32x4 __attribute__((ext_vector_type(4)));
typedef __bf16 bf16x2 __attribute__((ext_vector_type(2)));
typedef float f32x2 __attribute__((ext_vector_type(2)));
typedef short bf16x8 __attribute__((ext_vector_type(8)));
typedef unsigned long long u64;
typedef GAS u64 gu64;
typedef GAS unsigned gu32;

constexpr int NB = 256, NT = 512, NW = 8;
constexpr int D = 512, HQ = 32, HKV = 8, SK = 32768, DH = 128, FF = 1792;
constexpr int NSPLIT = 32, KEYS_PER_BLOCK = SK / NSPLIT;
constexpr int NTILE = KEYS_PER_BLOCK / 16 / NW;
constexpr int NMLP = 224;
constexpr float C2 = 0.088388347648318440f * 1.4426950408889634f;
constexpr int WOFF = 16, TSTR = 128;
#define POLL_SLEEP 4
#define QHI 1
#define QPRIO 1
#define QDELAY 4
constexpr unsigned kSpinCap = 1u << 15;
constexpr int AUX_SC1 = 16;
constexpr int RSRC_W3 = 0x00020000;

constexpr int WS_NONCE = 0;
constexpr int WS_QG = 4096;
constexpr int WS_HG = WS_QG + 8 * 4 * 512 * 8;
constexpr int WS_AML = 262144;
constexpr int WS_AACC = WS_AML + 32 * 32 * 16;
constexpr int WS_SG = WS_AACC + 32 * 32 * 1024;
constexpr int WS_MG = WS_SG + 8 * 32 * 64 * 8;
constexpr int WS_END = WS_MG + 8 * 28 * 64 * 8;
constexpr int L_XS0 = 0, L_XS1 = 512, L_QS = 1024, L_FLAG = 1280, L_P = 1296, L_MRG = L_P + NW * 64, MRG_REC = 132;
constexpr int L_H = L_MRG + NW * 4 * MRG_REC, L_T1 = L_H + 64, L_T2 = L_T1 + NW * MRG_REC, L_T3 = L_T2 + NW * 64, L_QP = L_T3 + NW * 64, L_HP = L_QP + 4 * 64, L_END = L_HP + 4 * 64;

struct Params {
  const uint16_t* x; const uint16_t* kc; const uint16_t* vc; const uint16_t* wq; const uint16_t* wo;
  const uint16_t* w1; const uint16_t* b1; const uint16_t* w2; const uint16_t* b2;
  uint16_t* out; unsigned char* ws;
};

__device__ __forceinline__ float bfr(float x) { return (float)(__bf16)x; }
__device__ __forceinline__ unsigned bfbits(float x) { return (unsigned)__builtin_bit_cast(unsigned short, (__bf16)x); }
__device__ __forceinline__ float lo16(unsigned u) { return __builtin_bit_cast(float, u << 16); }
__device__ __forceinline__ float hi16(unsigned u) { return __builtin_bit_cast(float, u & 0xffff0000u); }
__device__ __forceinline__ float dot2(unsigned k, unsigned q, float acc) {
  return __builtin_amdgcn_fdot2_f32_bf16(__builtin_bit_cast(bf16x2, k), __builtin_bit_cast(bf16x2, q), acc, false);
}
template <int CTRL> __device__ __forceinline__ float dpp(float x) {
  return __builtin_bit_cast(float, __builtin_amdgcn_mov_dpp(__builtin_bit_cast(int, x), CTRL, 0xf, 0xf, true));
}
constexpr int XOR1 = 0xB1, XOR2 = 0x4E, XOR7 = 0x141, XOR15 = 0x140, XOR8 = 0x128, ROR4 = 0x124;
__device__ __forceinline__ float xrow16_max(float x) {
  auto s = __builtin_amdgcn_permlane16_swap(__float_as_uint(x), __float_as_uint(x), false, false);
  x = fmaxf(__uint_as_float(s[0]), __uint_as_float(s[1]));
  auto t = __builtin_amdgcn_permlane32_swap(__float_as_uint(x), __float_as_uint(x), false, false);
  return fmaxf(__uint_as_float(t[0]), __uint_as_float(t[1]));
}
__device__ __forceinline__ float xrow16_sum(float x) {
  auto s = __builtin_amdgcn_permlane16_swap(__float_as_uint(x), __float_as_uint(x), false, false);
  x = __uint_as_float(s[0]) + __uint_as_float(s[1]);
  auto t = __builtin_amdgcn_permlane32_swap(__float_as_uint(x), __float_as_uint(x), false, false);
  return __uint_as_float(t[0]) + __uint_as_float(t[1]);
}
__device__ __forceinline__ float wave_sum(float x) {
  x += dpp<XOR1>(x); x += dpp<XOR2>(x); x += dpp<XOR7>(x); x += dpp<XOR15>(x);
  return xrow16_sum(x);
}
__device__ __forceinline__ void layernorm8(float (&v)[8]) {
  float s = 0.f;
#pragma unroll
  for (int i = 0; i < 8; ++i) s += v[i];
  const float mean = wave_sum(s) * (1.f / 512.f);
  float q = 0.f;
#pragma unroll
  for (int i = 0; i < 8; ++i) { const float d = v[i] - mean; q = fmaf(d, d, q); }
  const float var = wave_sum(q) * (1.f / 512.f);
  const float mb = bfr(mean), vb = bfr(var);
  const float den = bfr(sqrtf(bfr(vb + 1e-5f)));
#pragma unroll
  for (int i = 0; i < 8; ++i) v[i] = bfr(bfr(v[i] - mb) / den);
}
__device__ __forceinline__ void unpack8(u32x4 w, float (&f)[8]) {
#pragma unroll
  for (int k = 0; k < 4; ++k) { f[2 * k] = lo16(w[k]); f[2 * k + 1] = hi16(w[k]); }
}
__device__ __forceinline__ float sel8(const float (&a)[8], int i) {
  float a0 = a[0], a1 = a[1], a2 = a[2], a3 = a[3], a4 = a[4], a5 = a[5], a6 = a[6], a7 = a[7];
  asm volatile("" : "+v"(a0), "+v"(a1), "+v"(a2), "+v"(a3), "+v"(a4), "+v"(a5), "+v"(a6), "+v"(a7));
  const bool b0 = (i & 1) != 0, b1 = (i & 2) != 0, b2 = (i & 4) != 0;
  const float r0 = b0 ? a1 : a0, r1 = b0 ? a3 : a2, r2 = b0 ? a5 : a4, r3 = b0 ? a7 : a6;
  const float s0 = b1 ? r1 : r0, s1 = b1 ? r3 : r2;
  return b2 ? s1 : s0;
}
__device__ __forceinline__ u32x4 ldnt(const void* p) { return __builtin_nontemporal_load((const u32x4*)p); }
__device__ __forceinline__ u32x4 ldpl(const void* p) { return __builtin_nontemporal_load((const u32x4*)p); }
#define LDS_WAIT() asm volatile("s_waitcnt lgkmcnt(0)" ::: "memory")

__global__ void __launch_bounds__(NT, 2) mega(Params p) {
  __shared__ __attribute__((aligned(16))) float lds[L_END];
  LAS unsigned* ldsu = (LAS unsigned*)lds;
  const int b = blockIdx.x, tid = threadIdx.x, lane = tid & 63;
  const int wave = __builtin_amdgcn_readfirstlane(tid >> 6);
  const int g = b & 7, sp = b >> 3;
  const int r = lane >> 4, c = lane & 15;
  const __amdgpu_buffer_rsrc_t rs = __builtin_amdgcn_make_buffer_rsrc((void*)p.ws, 0, WS_END, RSRC_W3);
  if (tid < 4) ldsu[L_FLAG + tid] = 0u;
  __syncthreads();

  const int hl = sp & 3, cg = sp >> 2, head1 = g * 4 + hl;
  const bool mlp = b < NMLP;
  const int hs = mlp ? (b & 7) * 28 + (b >> 3) : 0;
  const int rg2 = mlp ? (b >> 3) : 0;
  const bool qw = QHI ? wave >= 4 : wave < 4;
  const int qi = wave & 3;
  const uint16_t* kb = p.kc + ((size_t)g * SK + (size_t)sp * KEYS_PER_BLOCK + wave * WOFF + c) * DH + 8 * r;
  const uint16_t* vb = p.vc + ((size_t)g * SK + (size_t)sp * KEYS_PER_BLOCK + wave * WOFF + 4 * r) * DH + 8 * c;
  u32x4 Kw[4][4], Vw[4][4], w2v, wo2[2];
#define ISSUE_K(t) do { _Pragma("unroll") for (int i = 0; i < 4; ++i) Kw[t][i] = ldnt(kb + (size_t)(t) * TSTR * DH + 32 * i); __builtin_amdgcn_sched_barrier(0); } while (0)
#define ISSUE_V(t) do { _Pragma("unroll") for (int i = 0; i < 4; ++i) Vw[t][i] = ldnt(vb + (size_t)((t) * TSTR + i) * DH); __builtin_amdgcn_sched_barrier(0); } while (0)
  if (qw) {
    const u64 nonce = *(const GAS u64*)(p.ws + WS_NONCE);
    __builtin_amdgcn_s_setprio(3);
    const int jc = sp & 7, ks = sp >> 3;
    const int j1 = mlp ? b % 28 : 0, k1 = mlp ? b / 28 : 0;
    const u32x4 xv = *(const GAS u32x4*)(p.x + 8 * lane);
    u32x4 wqv[4], w1v[2];
#pragma unroll
    for (int j = 0; j < 4; ++j) wqv[j] = ldpl(p.wq + (size_t)(128 * ks + 32 * qi + 8 * j + (lane >> 3)) * 4096 + g * 512 + jc * 64 + (lane & 7) * 8);
    __builtin_amdgcn_sched_barrier(0);
    __builtin_amdgcn_s_barrier();
    __builtin_amdgcn_sched_barrier(0);
#pragma unroll
    for (int j = 0; j < 2; ++j) w1v[j] = ldpl(p.w1 + (size_t)(64 * k1 + 16 * qi + 8 * j + (lane >> 3)) * FF + j1 * 64 + (lane & 7) * 8);
    __builtin_amdgcn_sched_barrier(0);
    const u64 tg = (u64)(unsigned)(nonce + 1ull) << 32;
    float v[8];
    unpack8(xv, v);
    layernorm8(v);
    *(LAS f32x4*)(lds + L_XS0 + 8 * lane) = f32x4{v[0], v[1], v[2], v[3]};
    *(LAS f32x4*)(lds + L_XS0 + 8 * lane + 4) = f32x4{v[4], v[5], v[6], v[7]};
    LDS_WAIT();
    float a[8];
#pragma unroll
    for (int k = 0; k < 8; ++k) a[k] = 0.f;
#pragma unroll
    for (int j = 0; j < 4; ++j) {
      const float xr = lds[L_XS0 + 128 * ks + 32 * qi + 8 * j + (lane >> 3)];
      float f[8]; unpack8(wqv[j], f);
#pragma unroll
      for (int k = 0; k < 8; ++k) a[k] = fmaf(xr, f[k], a[k]);
    }
#pragma unroll
    for (int k = 0; k < 8; ++k) { float t = a[k]; t += dpp<XOR8>(t); a[k] = xrow16_sum(t); }
    lds[L_QP + qi * 64 + (lane & 7) * 8 + (lane >> 3)] = sel8(a, lane >> 3);
    LDS_WAIT();
    asm volatile("" :: "v"((unsigned)(tg >> 32)) : "memory");
    unsigned old = 0;
    if (lane == 0) old = __hip_atomic_fetch_add(ldsu + L_FLAG + 2, 1u, __ATOMIC_RELAXED, __HIP_MEMORY_SCOPE_WORKGROUP);
    old = __builtin_amdgcn_readfirstlane(old);
    asm volatile("" ::: "memory");
    if (old == 3) {
      float qv = 0.f;
#pragma unroll
      for (int w = 0; w < 4; ++w) qv += lds[L_QP + w * 64 + lane];
      __hip_atomic_store((gu64*)(p.ws + WS_QG + ((g * 4 + ks) * 512 + jc * 64 + lane) * 8), tg | (u64)__float_as_uint(qv), RLX_AGENT);
    }
    if (qi == 0) {
      const unsigned t32 = (unsigned)(tg >> 32);
#pragma unroll
      for (int z = 0; z < QDELAY; ++z) __builtin_amdgcn_s_sleep(16);
      for (unsigned spins = 0;; ++spins) {
        u32x4 qg[4][4];
#pragma unroll
        for (int i = 0; i < 4; ++i)
#pragma unroll
          for (int k = 0; k < 4; ++k) qg[i][k] = __builtin_amdgcn_raw_buffer_load_b128(rs, WS_QG + ((g * 4 + k) * 512 + i * 128 + lane * 2) * 8, 0, AUX_SC1);
        bool ok = true;
#pragma unroll
        for (int i = 0; i < 4; ++i)
#pragma unroll
          for (int k = 0; k < 4; ++k) ok &= (qg[i][k][1] == t32) & (qg[i][k][3] == t32);
        if (__all(ok) || spins > kSpinCap) {
#pragma unroll
          for (int i = 0; i < 4; ++i) {
            const float q0 = (__uint_as_float(qg[i][0][0]) + __uint_as_float(qg[i][1][0])) + (__uint_as_float(qg[i][2][0]) + __uint_as_float(qg[i][3][0]));
            const float q1 = (__uint_as_float(qg[i][0][2]) + __uint_as_float(qg[i][1][2])) + (__uint_as_float(qg[i][2][2]) + __uint_as_float(qg[i][3][2]));
            ldsu[L_QS + i * 64 + lane] = bfbits(q0) | (bfbits(q1) << 16);
          }
          break;
        }
        __builtin_amdgcn_s_sleep(2);
      }
      if (lane == 0) { ldsu[L_FLAG + 4] = (unsigned)nonce; ldsu[L_FLAG + 5] = (unsigned)(nonce >> 32); }
      LDS_WAIT();
      if (lane == 0) __hip_atomic_store(ldsu + L_FLAG, 1u, __ATOMIC_RELAXED, __HIP_MEMORY_SCOPE_WORKGROUP);
    }
    layernorm8(v);
    *(LAS f32x4*)(lds + L_XS1 + 8 * lane) = f32x4{v[0], v[1], v[2], v[3]};
    *(LAS f32x4*)(lds + L_XS1 + 8 * lane + 4) = f32x4{v[4], v[5], v[6], v[7]};
    LDS_WAIT();
    {
      const float y0 = lds[L_XS1 + 64 * k1 + 16 * qi + (lane >> 3)], y1 = lds[L_XS1 + 64 * k1 + 16 * qi + 8 + (lane >> 3)];
      float f0[8], f1[8];
      unpack8(w1v[0], f0); unpack8(w1v[1], f1);
#pragma unroll
      for (int k = 0; k < 8; ++k) { float t = fmaf(y1, f1[k], y0 * f0[k]); t += dpp<XOR8>(t); a[k] = xrow16_sum(t); }
      lds[L_HP + qi * 64 + (lane & 7) * 8 + (lane >> 3)] = sel8(a, lane >> 3);
      LDS_WAIT();
      if (lane == 0) old = __hip_atomic_fetch_add(ldsu + L_FLAG + 1, 1u, __ATOMIC_RELAXED, __HIP_MEMORY_SCOPE_WORKGROUP);
      old = __builtin_amdgcn_readfirstlane(old);
      asm volatile("" ::: "memory");
      if (mlp && old == 3) {
        float hp = 0.f;
#pragma unroll
        for (int w = 0; w < 4; ++w) hp += lds[L_HP + w * 64 + lane];
        __hip_atomic_store((gu64*)(p.ws + WS_HG + ((j1 * 8 + k1) * 64 + lane) * 8), tg | (u64)__float_as_uint(hp), RLX_AGENT);
      }
    }
    __builtin_amdgcn_s_setprio(0);
  } else {
    __builtin_amdgcn_s_barrier();
  }
  __builtin_amdgcn_sched_barrier(0);
  ISSUE_K(0); ISSUE_V(0); ISSUE_K(1); ISSUE_V(1); ISSUE_K(2); ISSUE_V(2); ISSUE_K(3); ISSUE_V(3);
#pragma unroll
  for (int j = 0; j < 2; ++j) wo2[j] = ldpl(p.wo + (size_t)(head1 * 128 + 16 * wave + 8 * j + (lane >> 3)) * D + cg * 64 + (lane & 7) * 8);
  w2v = ldpl(p.w2 + (size_t)(64 * rg2 + 8 * wave + (lane >> 3)) * D + 64 * (b & 7) + (lane & 7) * 8);
  const unsigned b2v = p.b2[cg * 64 + lane], xo = p.x[cg * 64 + lane];
  __builtin_amdgcn_sched_barrier(0);
  for (unsigned spins = 0; spins < (1u << 24); ++spins) {
    if (__hip_atomic_load(ldsu + L_FLAG, __ATOMIC_RELAXED, __HIP_MEMORY_SCOPE_WORKGROUP) != 0u) break;
    __builtin_amdgcn_s_sleep(1);
  }
  asm volatile("" ::: "memory");
  const u64 nonce = (u64)ldsu[L_FLAG + 4] | ((u64)ldsu[L_FLAG + 5] << 32);
  const unsigned tag32 = (unsigned)(nonce + 1ull);
  const u64 tag32h = (u64)tag32 << 32;
  u32x4 qf[4];
#pragma unroll
  for (int j = 0; j < 4; ++j) {
    const u32x4 t = *(LAS u32x4*)(ldsu + L_QS + (c & 3) * 64 + 16 * j + 4 * r);
    qf[j] = (c < 4) ? t : u32x4{0u, 0u, 0u, 0u};
  }

  float m = -INFINITY, l = 0.f;
  f32x2 acc[16];
#pragma unroll
  for (int k = 0; k < 16; ++k) acc[k] = f32x2{0.f, 0.f};
  float* pw = lds + L_P + wave * 64;
#pragma unroll
  for (int t = 0; t < NTILE; ++t) {
    const int sl = t & 3;
    f32x4 d = {0.f, 0.f, 0.f, 0.f};
#pragma unroll
    for (int j = 0; j < 4; ++j)
      d = __builtin_amdgcn_mfma_f32_16x16x32_bf16(__builtin_bit_cast(bf16x8, Kw[sl][j]), __builtin_bit_cast(bf16x8, qf[j]), d, 0, 0, 0);
    __builtin_amdgcn_sched_barrier(0);
    if (t + 4 < NTILE) {
#pragma unroll
      for (int i = 0; i < 4; ++i) Kw[sl][i] = ldnt(kb + (size_t)(t + 4) * TSTR * DH + 32 * i);
    }
    __builtin_amdgcn_sched_barrier(0);
    const float s0 = d[0] * C2, s1 = d[1] * C2, s2 = d[2] * C2, s3 = d[3] * C2;
    const float pm = xrow16_max(fmaxf(fmaxf(s0, s1), fmaxf(s2, s3)));
    const float mn = fmaxf(m, pm), msub = (mn == -INFINITY) ? 0.f : mn;
    const float alpha = __builtin_amdgcn_exp2f(m - msub);
    const f32x4 pv = {__builtin_amdgcn_exp2f(s0 - msub), __builtin_amdgcn_exp2f(s1 - msub), __builtin_amdgcn_exp2f(s2 - msub), __builtin_amdgcn_exp2f(s3 - msub)};
    l = fmaf(l, alpha, (pv[0] + pv[1]) + (pv[2] + pv[3])); m = mn;
    if (c < 4) *(LAS f32x4*)(pw + c * 16 + 4 * r) = pv;
#pragma unroll
    for (int h = 0; h < 4; ++h) {
      const float ah = __builtin_bit_cast(float, __builtin_amdgcn_readlane(__builtin_bit_cast(int, alpha), h));
#pragma unroll
      for (int k = 0; k < 4; ++k) acc[h * 4 + k] *= f32x2{ah, ah};
    }
    LDS_WAIT();
    f32x4 pr[4];
#pragma unroll
    for (int h = 0; h < 4; ++h) pr[h] = *(LAS f32x4*)(pw + h * 16 + 4 * r);
#pragma unroll
    for (int i = 0; i < 4; ++i) {
      f32x2 vf[4];
#pragma unroll
      for (int k = 0; k < 4; ++k) vf[k] = f32x2{lo16(Vw[sl][i][k]), hi16(Vw[sl][i][k])};
#pragma unroll
      for (int h = 0; h < 4; ++h) {
        const f32x2 pg = {pr[h][i], pr[h][i]};
#pragma unroll
        for (int k = 0; k < 4; ++k) acc[h * 4 + k] = __builtin_elementwise_fma(pg, vf[k], acc[h * 4 + k]);
      }
    }
    asm volatile("" : "+v"(acc[0]), "+v"(acc[1]), "+v"(acc[2]), "+v"(acc[3]), "+v"(acc[4]), "+v"(acc[5]), "+v"(acc[6]), "+v"(acc[7]),
                      "+v"(acc[8]), "+v"(acc[9]), "+v"(acc[10]), "+v"(acc[11]), "+v"(acc[12]), "+v"(acc[13]), "+v"(acc[14]), "+v"(acc[15]));
    __builtin_amdgcn_sched_barrier(0);
    if (t + 4 < NTILE) {
#pragma unroll
      for (int i = 0; i < 4; ++i) Vw[sl][i] = ldnt(vb + (size_t)((t + 4) * TSTR + i) * DH);
    }
    __builtin_amdgcn_sched_barrier(0);
  }
#pragma unroll
  for (int k = 0; k < 16; ++k) acc[k] = f32x2{xrow16_sum(acc[k][0]), xrow16_sum(acc[k][1])};
  l = xrow16_sum(l);
  if (r == 0) {
#pragma unroll
    for (int h = 0; h < 4; ++h) {
      float* rec = lds + L_MRG + (wave * 4 + h) * MRG_REC;
      *(LAS f32x4*)(rec + 4 + 8 * c) = f32x4{acc[h * 4][0], acc[h * 4][1], acc[h * 4 + 1][0], acc[h * 4 + 1][1]};
      *(LAS f32x4*)(rec + 4 + 8 * c + 4) = f32x4{acc[h * 4 + 2][0], acc[h * 4 + 2][1], acc[h * 4 + 3][0], acc[h * 4 + 3][1]};
      if (c == h) { rec[0] = m; rec[1] = l; }
    }
  }
  __syncthreads();
  if (wave < 4) {
    const int h = wave;
    float M = -INFINITY;
#pragma unroll
    for (int w = 0; w < NW; ++w) M = fmaxf(M, lds[L_MRG + (w * 4 + h) * MRG_REC]);
    float L = 0.f, o0 = 0.f, o1 = 0.f;
#pragma unroll
    for (int w = 0; w < NW; ++w) {
      const float* rec = lds + L_MRG + (w * 4 + h) * MRG_REC;
      const float wt = __builtin_amdgcn_exp2f(rec[0] - M);
      L = fmaf(wt, rec[1], L); o0 = fmaf(wt, rec[4 + 2 * lane], o0); o1 = fmaf(wt, rec[5 + 2 * lane], o1);
    }
    const int slot = (g * 4 + h) * 32 + sp;
    __builtin_amdgcn_raw_buffer_store_b128(u32x4{__float_as_uint(o0), tag32, __float_as_uint(o1), tag32}, rs, WS_AACC + slot * 1024 + lane * 16, 0, AUX_SC1);
    if (lane == 0) __builtin_amdgcn_raw_buffer_store_b128(u32x4{__float_as_uint(M), tag32, __float_as_uint(L), tag32}, rs, WS_AML + slot * 16, 0, AUX_SC1);
  }
  if (wave == 7 && mlp) {
    const unsigned b1v = __builtin_nontemporal_load(p.b1 + 64 * rg2 + lane);
    for (unsigned spins = 0;; ++spins) {
      u64 hg[8];
#pragma unroll
      for (int k = 0; k < 8; ++k) hg[k] = __hip_atomic_load((gu64*)(p.ws + WS_HG + ((rg2 * 8 + k) * 64 + lane) * 8), RLX_AGENT);
      bool ok = true;
#pragma unroll
      for (int k = 0; k < 8; ++k) ok &= (unsigned)(hg[k] >> 32) == tag32;
      if (__all(ok) || spins > kSpinCap) {
        float hp = 0.f;
#pragma unroll
        for (int k = 0; k < 8; ++k) hp += __uint_as_float((unsigned)hg[k]);
        const float u = bfr(bfr(hp) + lo16(b1v));
        const float inner = 0.7978845608028654f * (u + 0.044715f * u * u * u);
        lds[L_H + lane] = bfr(0.5f * u * (1.f + tanhf(inner)));
        break;
      }
      __builtin_amdgcn_s_sleep(1);
    }
  }
  const bool is_final = (hl == 0) && (g == cg);
  {
    float Mw, Lw, o0, o1;
    u32x4 mlv;
    for (unsigned spins = 0;; ++spins) {
      mlv = __builtin_amdgcn_raw_buffer_load_b128(rs, WS_AML + (head1 * 32 + wave * 4 + (lane & 3)) * 16, 0, AUX_SC1);
      if (__all((mlv[1] == tag32) & (mlv[3] == tag32)) || spins > kSpinCap) break;
      __builtin_amdgcn_s_sleep(POLL_SLEEP);
    }
    for (unsigned spins = 0;; ++spins) {
      u32x4 ac[4];
#pragma unroll
      for (int j = 0; j < 4; ++j) ac[j] = __builtin_amdgcn_raw_buffer_load_b128(rs, WS_AACC + (head1 * 32 + wave * 4 + j) * 1024 + lane * 16, 0, AUX_SC1);
      bool ok = true;
#pragma unroll
      for (int j = 0; j < 4; ++j) ok &= (ac[j][1] == tag32) & (ac[j][3] == tag32);
      if (__all(ok) || spins > kSpinCap) {
        float mj[4], lj[4];
#pragma unroll
        for (int j = 0; j < 4; ++j) {
          mj[j] = __builtin_bit_cast(float, __builtin_amdgcn_readlane((int)mlv[0], j));
          lj[j] = __builtin_bit_cast(float, __builtin_amdgcn_readlane((int)mlv[2], j));
        }
        Mw = fmaxf(fmaxf(mj[0], mj[1]), fmaxf(mj[2], mj[3]));
        Lw = 0.f; o0 = 0.f; o1 = 0.f;
#pragma unroll
        for (int j = 0; j < 4; ++j) {
          const float wt = __builtin_amdgcn_exp2f(mj[j] - Mw);
          Lw = fmaf(wt, lj[j], Lw);
          o0 = fmaf(wt, __uint_as_float(ac[j][0]), o0); o1 = fmaf(wt, __uint_as_float(ac[j][2]), o1);
        }
        break;
      }
      __builtin_amdgcn_s_sleep(1);
    }
    float* rec = lds + L_T1 + wave * MRG_REC;
    rec[4 + 2 * lane] = o0; rec[5 + 2 * lane] = o1;
    if (lane == 0) { rec[0] = Mw; rec[1] = Lw; }
  }
  __syncthreads();
  {
    float M = -INFINITY;
#pragma unroll
    for (int w = 0; w < NW; ++w) M = fmaxf(M, lds[L_T1 + w * MRG_REC]);
    float L = 0.f, oa = 0.f, ob = 0.f;
    const int ra = 16 * wave + (lane >> 3), rb = ra + 8;
#pragma unroll
    for (int w = 0; w < NW; ++w) {
      const float* rec = lds + L_T1 + w * MRG_REC;
      const float wt = __builtin_amdgcn_exp2f(rec[0] - M);
      L = fmaf(wt, rec[1], L); oa = fmaf(wt, rec[4 + ra], oa); ob = fmaf(wt, rec[4 + rb], ob);
    }
    const float inv = 1.f / L;
    oa = bfr(oa * inv); ob = bfr(ob * inv);
    float fa[8], fb[8], a[8];
    unpack8(wo2[0], fa); unpack8(wo2[1], fb);
#pragma unroll
    for (int k = 0; k < 8; ++k) { float t = fmaf(ob, fb[k], oa * fa[k]); t += dpp<XOR8>(t); a[k] = xrow16_sum(t); }
    lds[L_T2 + wave * 64 + (lane & 7) * 8 + (lane >> 3)] = sel8(a, lane >> 3);
    const float hr = lds[L_H + 8 * wave + (lane >> 3)];
    unpack8(w2v, fa);
#pragma unroll
    for (int k = 0; k < 8; ++k) { float t = hr * fa[k]; t += dpp<XOR8>(t); a[k] = xrow16_sum(t); }
    lds[L_T3 + wave * 64 + (lane & 7) * 8 + (lane >> 3)] = sel8(a, lane >> 3);
  }
  __syncthreads();
  if (wave == 0) {
    float s = 0.f;
#pragma unroll
    for (int w = 0; w < NW; ++w) s += lds[L_T2 + w * 64 + lane];
    __hip_atomic_store((gu64*)(p.ws + WS_SG + ((cg * 32 + head1) * 64 + lane) * 8), tag32h | (u64)__float_as_uint(s), RLX_AGENT);
  } else if (wave == 1 && mlp) {
    float s = 0.f;
#pragma unroll
    for (int w = 0; w < NW; ++w) s += lds[L_T3 + w * 64 + lane];
    __hip_atomic_store((gu64*)(p.ws + WS_MG + (((b & 7) * 28 + (b >> 3)) * 64 + lane) * 8), tag32h | (u64)__float_as_uint(s), RLX_AGENT);
  }
  if (b == NB - 1 && wave == 7) {
    for (unsigned spins = 0;; ++spins) {
      bool ok = true;
#pragma unroll
      for (int j = 0; j < 4; ++j) {
        const u64 q = __hip_atomic_load((gu64*)(p.ws + WS_QG + (j * 64 + lane) * 64 * 8), RLX_AGENT);
        ok &= (unsigned)(q >> 32) == tag32;
      }
      if (__all(ok) || spins > kSpinCap) break;
      __builtin_amdgcn_s_sleep(1);
    }
    if (lane == 0) __hip_atomic_store((gu64*)(p.ws + WS_NONCE), nonce + 1ull, RLX_AGENT);
  }
  if (!is_final) return;
  {
    float ss, mm;
    for (unsigned spins = 0;; ++spins) {
      u64 sv[4], mv[4];
#pragma unroll
      for (int j = 0; j < 4; ++j) {
        sv[j] = __hip_atomic_load((gu64*)(p.ws + WS_SG + ((cg * 32 + wave * 4 + j) * 64 + lane) * 8), RLX_AGENT);
        const int rg = wave + 8 * j;
        mv[j] = (rg < 28) ? __hip_atomic_load((gu64*)(p.ws + WS_MG + ((cg * 28 + rg) * 64 + lane) * 8), RLX_AGENT) : tag32h;
      }
      bool ok = true;
#pragma unroll
      for (int j = 0; j < 4; ++j) ok &= ((unsigned)(sv[j] >> 32) == tag32) & ((unsigned)(mv[j] >> 32) == tag32);
      if (__all(ok) || spins > kSpinCap) {
        ss = 0.f; mm = 0.f;
#pragma unroll
        for (int j = 0; j < 4; ++j) { ss += __uint_as_float((unsigned)sv[j]); mm += __uint_as_float((unsigned)mv[j]); }
        break;
      }
      __builtin_amdgcn_s_sleep(1);
    }
    lds[L_MRG + wave * 128 + lane] = ss;
    lds[L_MRG + wave * 128 + 64 + lane] = mm;
  }
  __syncthreads();
  if (wave == 0) {
    float s = 0.f, mlp = 0.f;
#pragma unroll
    for (int w = 0; w < NW; ++w) { s += lds[L_MRG + w * 128 + lane]; mlp += lds[L_MRG + w * 128 + 64 + lane]; }
    const int col = cg * 64 + lane;
    const float sb = bfr(s);
    const float u = bfr(bfr(mlp) + lo16(b2v));
    const float gv = bfr(u / (1.f + __expf(-u)));
    const float o = lo16(xo) + bfr(sb * gv);
    p.out[col] = (uint16_t)bfbits(o);
  }
}

extern "C" void kernel_launch(void* const* d_in, const int* in_sizes, int n_in, void* d_out, int out_size, void* d_ws, size_t ws_size,
                              hipStream_t stream) {
  Params p;
  p.x = (const uint16_t*)d_in[0]; p.kc = (const uint16_t*)d_in[1]; p.vc = (const uint16_t*)d_in[2];
  p.wq = (const uint16_t*)d_in[3]; p.wo = (const uint16_t*)d_in[4];
  p.w1 = (const uint16_t*)d_in[6]; p.b1 = (const uint16_t*)d_in[7]; p.w2 = (const uint16_t*)d_in[8]; p.b2 = (const uint16_t*)d_in[9];
  p.out = (uint16_t*)d_out; p.ws = (unsigned char*)d_ws;
  hipLaunchKernelGGL(mega, dim3(NB), dim3(NT), 0, stream, p);
}
```
